```python
import torch

def _softmax(x, axis=-1):
    return torch.softmax(x, dim=axis)
def _gelu(x):
    return torch.nn.functional.gelu(x, approximate="tanh")
def _silu(x):
    return torch.nn.functional.silu(x)
def _sigmoid(x):
    return torch.sigmoid(x)
def _layernorm(x, axis=-1, eps=1e-5):
    m = x.mean(dim=axis, keepdim=True)
    v = x.var(dim=axis, keepdim=True, unbiased=False)
    return (x - m) / torch.sqrt(v + eps)
def _scatter_add(tgt, idx, src, axis=0):
    return tgt.clone().index_add_(axis, idx, src)
def _segment_sum(src, seg, n):
    out = torch.zeros((n,) + src.shape[1:], dtype=src.dtype)
    return out.index_add_(0, seg, src)
def _pad(x, pw):
    flat = tuple(v for pair in reversed(pw) for v in pair)
    return torch.nn.functional.pad(x, flat)

kernel_name = 'standalone__gqa_block_cached__sampling'

def setup_inputs():

    _w = (lambda a: (lambda t: t.to(torch.bfloat16) if t.is_floating_point() else t)(torch.as_tensor(a)))
    B, Sq, D = 8, 4, 512
    Hq, Hkv, Sk, Dh = 32, 8, 4096, 128
    assert Hq % Hkv == 0, (Hq, Hkv)
    return {
        "x": _w((lambda *s: torch.randn(*s, dtype=torch.bfloat16) * 0.8)(B, Sq, D)),
        "k_cache": _w((lambda *s: torch.randn(*s, dtype=torch.bfloat16) * 0.8)(B, Hkv, Sk, Dh)),
        "v_cache": _w((lambda *s: torch.randn(*s, dtype=torch.bfloat16) * 0.05)(B, Hkv, Sk, Dh)),
        "wq": _w((lambda *s: torch.randn(*s, dtype=torch.bfloat16) * 0.8)(D, Hq * Dh) * (D ** -0.5)),
        "wo": _w((lambda *s: torch.randn(*s, dtype=torch.bfloat16) * 0.05)(Hq * Dh, D) * ((Hq * Dh) ** -0.5)),
    }

def reference(x, k_cache, v_cache, wq, wo):
    B, Sq, D = x.shape
    Hq, Dh = 32, 128
    Hkv = k_cache.shape[1]
    rep = Hq // Hkv
    q = (lambda x, *d: x.permute(*d))((x @ wq).reshape(B, Sq, Hq, Dh), 0, 2, 1, 3)
    k = (lambda x, n, axis: torch.repeat_interleave(x, n, dim=axis))(k_cache, rep, axis=1)
    v = (lambda x, n, axis: torch.repeat_interleave(x, n, dim=axis))(v_cache, rep, axis=1)
    scores = torch.einsum("bhqd,bhkd->bhqk", q, k) * (Dh ** -0.5)
    p = _softmax(scores, axis=-1)
    o = torch.einsum("bhqk,bhkd->bhqd", p, v)
    return (lambda x, *d: x.permute(*d))(o, 0, 2, 1, 3).reshape(B, Sq, Hq * Dh) @ wo
```

```cpp
#include <hip/hip_runtime.h>
#include <hip/hip_bf16.h>
#include <stdint.h>

typedef __attribute__((ext_vector_type(8))) short bf16x8;
typedef __attribute__((ext_vector_type(4))) float f32x4;
typedef __attribute__((ext_vector_type(2))) float f32x2;
typedef __attribute__((ext_vector_type(2))) __bf16 bf16x2_t;
typedef __attribute__((ext_vector_type(4))) unsigned u32x4;
typedef __attribute__((ext_vector_type(2))) unsigned u32x2;
typedef unsigned short u16;

constexpr int NB = 8, SQ = 4, DM = 512, HQ = 32, HKV = 8, SK = 4096, DH = 128;
constexpr int GRP = HQ / HKV;
constexpr int MROW = GRP * SQ;
constexpr int NSPLIT = 4;
constexpr int KEYS_WG = SK / NSPLIT;
constexpr int NWAVE = 8;
constexpr int TILES_W = KEYS_WG / 32 / NWAVE;
constexpr float SCALE_LOG2E = 0.08838834764831845f * 1.4426950408889634f;

constexpr size_t WS_CNT = 0;
constexpr size_t WS_Q = 4096;
constexpr size_t WS_ML = WS_Q + (size_t)NB * HKV * MROW * DH * 2;
constexpr size_t WS_PO = WS_ML + (size_t)NB * HKV * NSPLIT * MROW * 2 * 4;
constexpr size_t WS_CON = WS_PO + (size_t)NB * HKV * NSPLIT * MROW * DH * 4;
constexpr size_t WS_END = WS_CON + (size_t)NB * 4 * HKV * SQ * 128 * 4;

__device__ __forceinline__ unsigned cvt_pk_bf16(float lo, float hi) {
  f32x2 v = {lo, hi};
  return __builtin_bit_cast(unsigned, __builtin_convertvector(v, bf16x2_t));
}
__device__ __forceinline__ float bf16_to_f32(u16 h) { return __builtin_bit_cast(float, (unsigned)h << 16); }

__device__ __forceinline__ float xg_max(float x) {
  auto s = __builtin_amdgcn_permlane16_swap(__float_as_uint(x), __float_as_uint(x), false, false);
  x = fmaxf(__uint_as_float(s[0]), __uint_as_float(s[1]));
  auto t = __builtin_amdgcn_permlane32_swap(__float_as_uint(x), __float_as_uint(x), false, false);
  return fmaxf(__uint_as_float(t[0]), __uint_as_float(t[1]));
}
__device__ __forceinline__ float xg_sum(float x) {
  auto s = __builtin_amdgcn_permlane16_swap(__float_as_uint(x), __float_as_uint(x), false, false);
  x = __uint_as_float(s[0]) + __uint_as_float(s[1]);
  auto t = __builtin_amdgcn_permlane32_swap(__float_as_uint(x), __float_as_uint(x), false, false);
  return __uint_as_float(t[0]) + __uint_as_float(t[1]);
}

template <int I>
__device__ __forceinline__ bf16x8 frag_col(const u32x4 (&r)[8]) {
  constexpr unsigned sel = (I & 1) ? 0x07060302u : 0x05040100u;
  u32x4 f;
  f[0] = __builtin_amdgcn_perm(r[1][I >> 1], r[0][I >> 1], sel);
  f[1] = __builtin_amdgcn_perm(r[3][I >> 1], r[2][I >> 1], sel);
  f[2] = __builtin_amdgcn_perm(r[5][I >> 1], r[4][I >> 1], sel);
  f[3] = __builtin_amdgcn_perm(r[7][I >> 1], r[6][I >> 1], sel);
  return __builtin_bit_cast(bf16x8, f);
}

#define MFMA(a, b, c) __builtin_amdgcn_mfma_f32_16x16x32_bf16((a), (b), (c), 0, 0, 0)

__global__ __launch_bounds__(256) void qproj_kernel(const u16* __restrict__ x, const u16* __restrict__ wq, unsigned char* __restrict__ ws) {
  __shared__ float red[4][32][132];
  const int tid = threadIdx.x, lane = tid & 63, w = tid >> 6, c16 = lane & 15, g = lane >> 4;
  const int h = blockIdx.x;
  if (h == 0 && tid < 32) reinterpret_cast<unsigned*>(ws + WS_CNT)[tid] = 0u;
  u32x4 wt[4][8];
#pragma unroll
  for (int kk = 0; kk < 4; ++kk) {
    const int kt = w * 4 + kk;
#pragma unroll
    for (int t = 0; t < 8; ++t)
      wt[kk][t] = *reinterpret_cast<const u32x4*>(wq + (size_t)(kt * 32 + g * 8 + t) * (HQ * DH) + h * DH + c16 * 8);
  }
  f32x4 acc[2][8];
#pragma unroll
  for (int mt = 0; mt < 2; ++mt)
#pragma unroll
    for (int i = 0; i < 8; ++i) acc[mt][i] = f32x4{0.f, 0.f, 0.f, 0.f};
#pragma unroll
  for (int kk = 0; kk < 4; ++kk) {
    const int kt = w * 4 + kk;
    bf16x8 a0 = *reinterpret_cast<const bf16x8*>(x + (size_t)(c16) * DM + kt * 32 + g * 8);
    bf16x8 a1 = *reinterpret_cast<const bf16x8*>(x + (size_t)(16 + c16) * DM + kt * 32 + g * 8);
#define QP(I) { bf16x8 bb = frag_col<I>(wt[kk]); acc[0][I] = MFMA(a0, bb, acc[0][I]); acc[1][I] = MFMA(a1, bb, acc[1][I]); }
    QP(0) QP(1) QP(2) QP(3) QP(4) QP(5) QP(6) QP(7)
#undef QP
  }
#pragma unroll
  for (int mt = 0; mt < 2; ++mt)
#pragma unroll
    for (int r = 0; r < 4; ++r) {
      float* dst = &red[w][mt * 16 + g * 4 + r][c16 * 8];
      *reinterpret_cast<f32x4*>(dst) = f32x4{acc[mt][0][r], acc[mt][1][r], acc[mt][2][r], acc[mt][3][r]};
      *reinterpret_cast<f32x4*>(dst + 4) = f32x4{acc[mt][4][r], acc[mt][5][r], acc[mt][6][r], acc[mt][7][r]};
    }
  __syncthreads();
  {
    const int row = tid >> 3, c0 = (tid & 7) * 16;
    const int b = row >> 2, sq = row & 3;
    const int hkv = h >> 2, j = h & 3;
    u16* q = reinterpret_cast<u16*>(ws + WS_Q) + ((size_t)(b * HKV + hkv) * MROW + j * 4 + sq) * DH + c0;
    unsigned outp[8];
#pragma unroll
    for (int e = 0; e < 16; e += 2) {
      float v0 = red[0][row][c0 + e] + red[1][row][c0 + e] + red[2][row][c0 + e] + red[3][row][c0 + e];
      float v1 = red[0][row][c0 + e + 1] + red[1][row][c0 + e + 1] + red[2][row][c0 + e + 1] + red[3][row][c0 + e + 1];
      outp[e >> 1] = cvt_pk_bf16(v0, v1);
    }
    *reinterpret_cast<u32x4*>(q) = u32x4{outp[0], outp[1], outp[2], outp[3]};
    *reinterpret_cast<u32x4*>(q + 8) = u32x4{outp[4], outp[5], outp[6], outp[7]};
  }
}

__device__ __forceinline__ u32x4 ld_stream(const u16* p) { return __builtin_nontemporal_load(reinterpret_cast<const u32x4*>(p)); }

__global__ __launch_bounds__(512, 2) void attn_kernel(const u16* __restrict__ Kc, const u16* __restrict__ Vc, unsigned char* __restrict__ ws) {
  extern __shared__ float smem[];
  float* sm_o = smem;
  float* sm_m = smem + NWAVE * MROW * 132;
  float* sm_l = sm_m + NWAVE * MROW;
  const int tid = threadIdx.x, lane = tid & 63, w = tid >> 6, c16 = lane & 15, g = lane >> 4;
  const int bid = blockIdx.x, s = bid & (NSPLIT - 1), bh = bid / NSPLIT;
  const u16* Kp = Kc + ((size_t)bh * SK + s * KEYS_WG) * DH;
  const u16* Vp = Vc + ((size_t)bh * SK + s * KEYS_WG) * DH;

  u32x4 kb[2][8], vb[2][8];
  auto load_k = [&](u32x4 (&k)[8], int it) {
    const u16* base = Kp + (size_t)((it * NWAVE + w) * 32 + c16) * DH + g * 8;
#pragma unroll
    for (int u = 0; u < 2; ++u)
#pragma unroll
      for (int c = 0; c < 4; ++c) k[u * 4 + c] = ld_stream(base + u * 16 * DH + c * 32);
  };
  auto load_v = [&](u32x4 (&v)[8], int it) {
    const u16* base = Vp + (size_t)((it * NWAVE + w) * 32 + g * 4) * DH + c16 * 8;
#pragma unroll
    for (int t = 0; t < 8; ++t) v[t] = ld_stream(base + ((t >> 2) * 16 + (t & 3)) * DH);
  };
  load_k(kb[0], 0);
  load_v(vb[0], 0);
  load_k(kb[1], 1);
  load_v(vb[1], 1);

  bf16x8 qf[4];
  {
    const u16* q = reinterpret_cast<const u16*>(ws + WS_Q) + ((size_t)bh * MROW + c16) * DH + g * 8;
#pragma unroll
    for (int c = 0; c < 4; ++c) qf[c] = *reinterpret_cast<const bf16x8*>(q + c * 32);
  }

  float m_run = -INFINITY, l_run = 0.f;
  f32x4 o[8];
#pragma unroll
  for (int i = 0; i < 8; ++i) o[i] = f32x4{0.f, 0.f, 0.f, 0.f};

#pragma unroll
  for (int it = 0; it < TILES_W; ++it) {
    u32x4 (&k)[8] = kb[it & 1];
    u32x4 (&v)[8] = vb[it & 1];
    f32x4 s0 = f32x4{0.f, 0.f, 0.f, 0.f}, s1 = f32x4{0.f, 0.f, 0.f, 0.f};
#pragma unroll
    for (int c = 0; c < 4; ++c) {
      s0 = MFMA(__builtin_bit_cast(bf16x8, k[c]), qf[c], s0);
      s1 = MFMA(__builtin_bit_cast(bf16x8, k[4 + c]), qf[c], s1);
    }
    if (it + 2 < TILES_W) load_k(k, it + 2);
    float tmax = fmaxf(fmaxf(fmaxf(s0[0], s0[1]), fmaxf(s0[2], s0[3])), fmaxf(fmaxf(s1[0], s1[1]), fmaxf(s1[2], s1[3])));
    tmax = xg_max(tmax);
    const float m_new = fmaxf(m_run, tmax);
    const float alpha = __builtin_amdgcn_exp2f((m_run - m_new) * SCALE_LOG2E);
    const float mb = m_new * SCALE_LOG2E;
    m_run = m_new;
    float p[8];
#pragma unroll
    for (int r = 0; r < 4; ++r) {
      p[r] = __builtin_amdgcn_exp2f(fmaf(s0[r], SCALE_LOG2E, -mb));
      p[4 + r] = __builtin_amdgcn_exp2f(fmaf(s1[r], SCALE_LOG2E, -mb));
    }
    l_run = fmaf(l_run, alpha, ((p[0] + p[1]) + (p[2] + p[3])) + ((p[4] + p[5]) + (p[6] + p[7])));
    u32x4 pf = {cvt_pk_bf16(p[0], p[1]), cvt_pk_bf16(p[2], p[3]), cvt_pk_bf16(p[4], p[5]), cvt_pk_bf16(p[6], p[7])};
    const bf16x8 pb = __builtin_bit_cast(bf16x8, pf);
#define PV(I) { o[I] = o[I] * alpha; o[I] = MFMA(frag_col<I>(v), pb, o[I]); }
    PV(0) PV(1) PV(2) PV(3) PV(4) PV(5) PV(6) PV(7)
#undef PV
    if (it + 2 < TILES_W) load_v(v, it + 2);
  }

  l_run = xg_sum(l_run);
  if (lane < 16) { sm_m[w * MROW + lane] = m_run; sm_l[w * MROW + lane] = l_run; }
  __syncthreads();
  float M = -INFINITY;
#pragma unroll
  for (int ww = 0; ww < NWAVE; ++ww) M = fmaxf(M, sm_m[ww * MROW + c16]);
  const float f = __builtin_amdgcn_exp2f((m_run - M) * SCALE_LOG2E);
#pragma unroll
  for (int r = 0; r < 4; ++r) {
    float* dst = sm_o + (w * MROW + c16) * 132 + g * 32 + r * 8;
    *reinterpret_cast<f32x4*>(dst) = f32x4{o[0][r] * f, o[1][r] * f, o[2][r] * f, o[3][r] * f};
    *reinterpret_cast<f32x4*>(dst + 4) = f32x4{o[4][r] * f, o[5][r] * f, o[6][r] * f, o[7][r] * f};
  }
  __syncthreads();
  {
    const int q = tid >> 5, d4 = tid & 31;
    f32x4 acc = *reinterpret_cast<const f32x4*>(sm_o + q * 132 + d4 * 4);
#pragma unroll
    for (int ww = 1; ww < NWAVE; ++ww) acc += *reinterpret_cast<const f32x4*>(sm_o + (ww * MROW + q) * 132 + d4 * 4);
    float* po = reinterpret_cast<float*>(ws + WS_PO) + ((size_t)bid * MROW + q) * DH + d4 * 4;
    *reinterpret_cast<f32x4*>(po) = acc;
    if (d4 == 0) {
      float Mq = -INFINITY;
#pragma unroll
      for (int ww = 0; ww < NWAVE; ++ww) Mq = fmaxf(Mq, sm_m[ww * MROW + q]);
      float L = 0.f;
#pragma unroll
      for (int ww = 0; ww < NWAVE; ++ww) L = fmaf(sm_l[ww * MROW + q], __builtin_amdgcn_exp2f((sm_m[ww * MROW + q] - Mq) * SCALE_LOG2E), L);
      float2* pml = reinterpret_cast<float2*>(ws + WS_ML) + (size_t)bid * MROW + q;
      *pml = make_float2(Mq * SCALE_LOG2E, L);
    }
  }
}

__global__ __launch_bounds__(512) void outproj_kernel(const u16* __restrict__ wo, unsigned char* __restrict__ ws, u16* __restrict__ out) {
  __shared__ __attribute__((aligned(16))) u16 sA[4][520];
  __shared__ __attribute__((aligned(16))) float red[NWAVE][4][132];
  __shared__ unsigned s_last;
  const int tid = threadIdx.x, lane = tid & 63, w = tid >> 6, c16 = lane & 15, g = lane >> 4;
  const int bid = blockIdx.x;
  const int xcd = bid & 7, rest = bid >> 3, b = rest & 7, sl = (rest >> 3) * 8 + xcd;
  const int hkv = sl >> 2, ns = sl & 3;
  u32x4 wt[2][8];
#pragma unroll
  for (int kk = 0; kk < 2; ++kk)
#pragma unroll
    for (int t = 0; t < 8; ++t)
      wt[kk][t] = *reinterpret_cast<const u32x4*>(wo + (size_t)(hkv * 512 + (w * 2 + kk) * 32 + g * 8 + t) * DM + ns * 128 + c16 * 8);
  {
    const int q = tid >> 5, d4 = tid & 31;
    const size_t wg0 = (size_t)(b * HKV + hkv) * NSPLIT;
    const float2* pml = reinterpret_cast<const float2*>(ws + WS_ML);
    const float* po = reinterpret_cast<const float*>(ws + WS_PO);
    float2 ml[NSPLIT];
    f32x4 ov[NSPLIT];
#pragma unroll
    for (int sp = 0; sp < NSPLIT; ++sp) {
      ml[sp] = pml[(wg0 + sp) * MROW + q];
      ov[sp] = *reinterpret_cast<const f32x4*>(po + ((wg0 + sp) * MROW + q) * DH + d4 * 4);
    }
    float M = ml[0].x;
#pragma unroll
    for (int sp = 1; sp < NSPLIT; ++sp) M = fmaxf(M, ml[sp].x);
    float L = 0.f;
    f32x4 acc = f32x4{0.f, 0.f, 0.f, 0.f};
#pragma unroll
    for (int sp = 0; sp < NSPLIT; ++sp) {
      const float wgt = __builtin_amdgcn_exp2f(ml[sp].x - M);
      L = fmaf(wgt, ml[sp].y, L);
      acc += ov[sp] * wgt;
    }
    const float inv = 1.f / L;
    const int j = q >> 2, sq = q & 3;
    u32x2 pk = {cvt_pk_bf16(acc[0] * inv, acc[1] * inv), cvt_pk_bf16(acc[2] * inv, acc[3] * inv)};
    *reinterpret_cast<u32x2*>(&sA[sq][j * 128 + d4 * 4]) = pk;
  }
  __syncthreads();
  f32x4 acc[8];
#pragma unroll
  for (int i = 0; i < 8; ++i) acc[i] = f32x4{0.f, 0.f, 0.f, 0.f};
#pragma unroll
  for (int kk = 0; kk < 2; ++kk) {
    const bf16x8 a = *reinterpret_cast<const bf16x8*>(&sA[c16 & 3][(w * 2 + kk) * 32 + g * 8]);
#define OP(I) { acc[I] = MFMA(a, frag_col<I>(wt[kk]), acc[I]); }
    OP(0) OP(1) OP(2) OP(3) OP(4) OP(5) OP(6) OP(7)
#undef OP
  }
  if (g == 0) {
#pragma unroll
    for (int r = 0; r < 4; ++r) {
      float* dst = &red[w][r][c16 * 8];
      *reinterpret_cast<f32x4*>(dst) = f32x4{acc[0][r], acc[1][r], acc[2][r], acc[3][r]};
      *reinterpret_cast<f32x4*>(dst + 4) = f32x4{acc[4][r], acc[5][r], acc[6][r], acc[7][r]};
    }
  }
  __syncthreads();
  const int grp = b * 4 + ns;
  float* con = reinterpret_cast<float*>(ws + WS_CON);
  const int sq = tid >> 7, n = tid & 127;
  {
    float v = 0.f;
#pragma unroll
    for (int ww = 0; ww < NWAVE; ++ww) v += red[ww][sq][n];
    __hip_atomic_store(con + ((size_t)grp * HKV + hkv) * 512 + sq * 128 + n, v, __ATOMIC_RELAXED, __HIP_MEMORY_SCOPE_AGENT);
  }
  asm volatile("s_waitcnt vmcnt(0)" ::: "memory");
  __syncthreads();
  if (tid == 0) {
    unsigned* cnt = reinterpret_cast<unsigned*>(ws + WS_CNT) + grp;
    const unsigned old = __hip_atomic_fetch_add(cnt, 1u, __ATOMIC_RELAXED, __HIP_MEMORY_SCOPE_AGENT);
    const unsigned last = (old == (unsigned)(HKV - 1)) ? 1u : 0u;
    if (last) __builtin_amdgcn_fence(__ATOMIC_ACQUIRE, "agent");
    asm volatile("s_waitcnt vmcnt(0)" ::: "memory");
    s_last = last;
  }
  __syncthreads();
  if (s_last == 0u) return;
  {
    float v = 0.f;
#pragma unroll
    for (int hh = 0; hh < HKV; ++hh) v += con[((size_t)grp * HKV + hh) * 512 + sq * 128 + n];
    f32x2 t = {v, 0.f};
    out[(size_t)(b * SQ + sq) * DM + ns * 128 + n] = (u16)(__builtin_bit_cast(unsigned, __builtin_convertvector(t, bf16x2_t)) & 0xffffu);
  }
}

extern "C" void kernel_launch(void* const* d_in, const int* in_sizes, int n_in, void* d_out, int out_size, void* d_ws,
                              size_t ws_size, hipStream_t stream) {
  const u16* x = (const u16*)d_in[0];
  const u16* kc = (const u16*)d_in[1];
  const u16* vc = (const u16*)d_in[2];
  const u16* wq = (const u16*)d_in[3];
  const u16* wo = (const u16*)d_in[4];
  unsigned char* ws = (unsigned char*)d_ws;
  constexpr int ATT_LDS = (NWAVE * MROW * 132 + 2 * NWAVE * MROW) * 4;
  hipFuncSetAttribute((const void*)attn_kernel, hipFuncAttributeMaxDynamicSharedMemorySize, ATT_LDS);
  qproj_kernel<<<HQ, 256, 0, stream>>>(x, wq, ws);
  attn_kernel<<<NB * HKV * NSPLIT, 512, ATT_LDS, stream>>>(kc, vc, ws);
  outproj_kernel<<<256, 512, 0, stream>>>(wo, ws, (u16*)d_out);
}
```

```cpp
#include <hip/hip_runtime.h>
#include <hip/hip_bf16.h>
#include <stdint.h>

typedef __attribute__((ext_vector_type(8))) short bf16x8;
typedef __attribute__((ext_vector_type(4))) float f32x4;
typedef __attribute__((ext_vector_type(2))) float f32x2;
typedef __attribute__((ext_vector_type(2))) __bf16 bf16x2_t;
typedef __attribute__((ext_vector_type(4))) unsigned u32x4;
typedef __attribute__((ext_vector_type(2))) unsigned u32x2;
typedef unsigned short u16;

constexpr int NB = 8, SQ = 4, DM = 512, HQ = 32, HKV = 8, SK = 4096, DH = 128;
constexpr int MROW = 16;
constexpr int NSPLIT = 4;
constexpr int KEYS_WG = SK / NSPLIT;
constexpr int NWAVE = 8;
constexpr int TILES_W = KEYS_WG / 32 / NWAVE;
constexpr float SCALE_LOG2E = 0.08838834764831845f * 1.4426950408889634f;

constexpr size_t WS_FLAG = 0;
constexpr size_t WS_TICK = 8192;
constexpr int WS_ZERO_WORDS = (8192 + 2048) / 4;
constexpr size_t WS_Q = 16384;
constexpr size_t WS_ML = WS_Q + (size_t)NB * HKV * MROW * DH * 2;
constexpr size_t WS_PO = WS_ML + (size_t)NB * HKV * NSPLIT * MROW * 2 * 4;
constexpr size_t WS_CON = WS_PO + (size_t)NB * HKV * NSPLIT * MROW * DH * 4;

__device__ __forceinline__ unsigned cvt_pk_bf16(float lo, float hi) {
  f32x2 v = {lo, hi};
  return __builtin_bit_cast(unsigned, __builtin_convertvector(v, bf16x2_t));
}

__device__ __forceinline__ float xg_max(float x) {
  auto s = __builtin_amdgcn_permlane16_swap(__float_as_uint(x), __float_as_uint(x), false, false);
  x = fmaxf(__uint_as_float(s[0]), __uint_as_float(s[1]));
  auto t = __builtin_amdgcn_permlane32_swap(__float_as_uint(x), __float_as_uint(x), false, false);
  return fmaxf(__uint_as_float(t[0]), __uint_as_float(t[1]));
}
__device__ __forceinline__ float xg_sum(float x) {
  auto s = __builtin_amdgcn_permlane16_swap(__float_as_uint(x), __float_as_uint(x), false, false);
  x = __uint_as_float(s[0]) + __uint_as_float(s[1]);
  auto t = __builtin_amdgcn_permlane32_swap(__float_as_uint(x), __float_as_uint(x), false, false);
  return __uint_as_float(t[0]) + __uint_as_float(t[1]);
}

template <int I>
__device__ __forceinline__ bf16x8 frag_col(const u32x4 (&r)[8]) {
  constexpr unsigned sel = (I & 1) ? 0x07060302u : 0x05040100u;
  u32x4 f;
  f[0] = __builtin_amdgcn_perm(r[1][I >> 1], r[0][I >> 1], sel);
  f[1] = __builtin_amdgcn_perm(r[3][I >> 1], r[2][I >> 1], sel);
  f[2] = __builtin_amdgcn_perm(r[5][I >> 1], r[4][I >> 1], sel);
  f[3] = __builtin_amdgcn_perm(r[7][I >> 1], r[6][I >> 1], sel);
  return __builtin_bit_cast(bf16x8, f);
}

#define MFMA(a, b, c) __builtin_amdgcn_mfma_f32_16x16x32_bf16((a), (b), (c), 0, 0, 0)

template <int IMM>
__device__ __forceinline__ void gld_nt(u32x4& d, unsigned voff, const void* sbase) {
  asm volatile("global_load_dwordx4 %0, %1, %2 offset:%3 nt" : "=v"(d) : "v"(voff), "s"(sbase), "n"(IMM) : "memory");
}
template <int IMM>
__device__ __forceinline__ void gld(u32x4& d, unsigned voff, const void* sbase) {
  asm volatile("global_load_dwordx4 %0, %1, %2 offset:%3" : "=v"(d) : "v"(voff), "s"(sbase), "n"(IMM) : "memory");
}
template <int N> __device__ __forceinline__ void wait_vm() { asm volatile("s_waitcnt vmcnt(%0)" :: "n"(N) : "memory"); }
#define TIE8(a) asm volatile("" : "+v"(a[0]), "+v"(a[1]), "+v"(a[2]), "+v"(a[3]), "+v"(a[4]), "+v"(a[5]), "+v"(a[6]), "+v"(a[7]))
#define TIE4(a) asm volatile("" : "+v"(a[0]), "+v"(a[1]), "+v"(a[2]), "+v"(a[3]))

__device__ __forceinline__ void st_wt16(void* p, f32x4 v) {
  asm volatile("global_store_dwordx4 %0, %1, off sc1\n\ts_nop 1" :: "v"(p), "v"(v) : "memory");
}
__device__ __forceinline__ void st_wt8(void* p, f32x2 v) {
  asm volatile("global_store_dwordx2 %0, %1, off sc1\n\ts_nop 1" :: "v"(p), "v"(v) : "memory");
}
__device__ __forceinline__ void st_wt4(void* p, float v) {
  asm volatile("global_store_dword %0, %1, off sc1\n\ts_nop 1" :: "v"(p), "v"(v) : "memory");
}
__device__ __forceinline__ void ld_wt16(f32x4& d, const void* p) { asm volatile("global_load_dwordx4 %0, %1, off sc1" : "=v"(d) : "v"(p) : "memory"); }
__device__ __forceinline__ void ld_wt8(f32x2& d, const void* p) { asm volatile("global_load_dwordx2 %0, %1, off sc1" : "=v"(d) : "v"(p) : "memory"); }
__device__ __forceinline__ void ld_wt4(float& d, const void* p) { asm volatile("global_load_dword %0, %1, off sc1" : "=v"(d) : "v"(p) : "memory"); }

#define STAMP(i) do { } while (0)

__global__ __launch_bounds__(256) void qproj_kernel(const u16* __restrict__ x, const u16* __restrict__ wq, unsigned char* __restrict__ ws) {
  __shared__ __attribute__((aligned(16))) float red[4][2][64 * 4];
  const int tid = threadIdx.x, lane = tid & 63, w = tid >> 6, c16 = lane & 15, g = lane >> 4;
  const int bid = blockIdx.x;
  const int slab = (bid & 7) * 32 + (bid >> 3);
  const int n0 = slab * 16;
  {
    const int gid = bid * 256 + tid;
    if (gid < WS_ZERO_WORDS) reinterpret_cast<unsigned*>(ws)[gid] = 0u;
  }
  u16 wv[4][8];
#pragma unroll
  for (int ks = 0; ks < 4; ++ks)
#pragma unroll
    for (int j = 0; j < 8; ++j) wv[ks][j] = wq[(size_t)(w * 128 + ks * 32 + g * 8 + j) * (HQ * DH) + n0 + c16];
  bf16x8 xb[4][2];
#pragma unroll
  for (int ks = 0; ks < 4; ++ks)
#pragma unroll
    for (int mt = 0; mt < 2; ++mt) xb[ks][mt] = *reinterpret_cast<const bf16x8*>(x + (size_t)(mt * 16 + c16) * DM + w * 128 + ks * 32 + g * 8);
  f32x4 acc[2] = {f32x4{0.f, 0.f, 0.f, 0.f}, f32x4{0.f, 0.f, 0.f, 0.f}};
#pragma unroll
  for (int ks = 0; ks < 4; ++ks) {
    u32x4 a;
#pragma unroll
    for (int j = 0; j < 4; ++j) a[j] = (unsigned)wv[ks][2 * j] | ((unsigned)wv[ks][2 * j + 1] << 16);
    const bf16x8 ab = __builtin_bit_cast(bf16x8, a);
    acc[0] = MFMA(ab, xb[ks][0], acc[0]);
    acc[1] = MFMA(ab, xb[ks][1], acc[1]);
  }
  *reinterpret_cast<f32x4*>(&red[w][0][lane * 4]) = acc[0];
  *reinterpret_cast<f32x4*>(&red[w][1][lane * 4]) = acc[1];
  __syncthreads();
  {
    const int m = tid >> 3, np = tid & 7;
    const int mt = m >> 4, cm = m & 15, nl = np * 2, gg = nl >> 2, r = nl & 3;
    float v0 = 0.f, v1 = 0.f;
#pragma unroll
    for (int ww = 0; ww < 4; ++ww) {
      const f32x2 t = *reinterpret_cast<const f32x2*>(&red[ww][mt][(gg * 16 + cm) * 4 + r]);
      v0 += t[0]; v1 += t[1];
    }
    const int b = m >> 2, sq = m & 3;
    const int hkv = slab >> 5, j = (slab >> 3) & 3, d0 = (slab & 7) * 16;
    unsigned* q = reinterpret_cast<unsigned*>(ws + WS_Q) + ((((size_t)(b * HKV + hkv) * MROW + j * 4 + sq) * DH + d0 + nl) >> 1);
    *q = cvt_pk_bf16(v0, v1);
  }
}

__global__ __launch_bounds__(512, 2) void attn_kernel(const u16* __restrict__ Kc, const u16* __restrict__ Vc, const u16* __restrict__ wo,
                                                      unsigned char* __restrict__ ws, u16* __restrict__ out) {
  extern __shared__ __attribute__((aligned(16))) float smem[];
  float* sm_o = smem;
  float* sm_m = smem + NWAVE * MROW * 132;
  float* sm_l = sm_m + NWAVE * MROW;
  u16* sA = reinterpret_cast<u16*>(sm_l + NWAVE * MROW);
  unsigned* s_last = reinterpret_cast<unsigned*>(sA + 4 * 520);

  const int tid = threadIdx.x, lane = tid & 63, c16 = lane & 15, g = lane >> 4;
  const int w = __builtin_amdgcn_readfirstlane(tid >> 6);
  const int bid = blockIdx.x;
  const int hkv = bid & 7, b = bid >> 5, s = (bid >> 3) & 3;
  const int bh = b * HKV + hkv;
  STAMP(0);
  const unsigned char* Kp = reinterpret_cast<const unsigned char*>(Kc + ((size_t)bh * SK + s * KEYS_WG) * DH) + w * 8192;
  const unsigned char* Vp = reinterpret_cast<const unsigned char*>(Vc + ((size_t)bh * SK + s * KEYS_WG) * DH) + w * 8192;
  const unsigned char* Qp = ws + WS_Q + (size_t)bh * MROW * DH * 2;
  const unsigned vk0 = c16 * 256 + g * 16, vk1 = vk0 + 4096;
  const unsigned vv0 = g * 1024 + c16 * 16, vv1 = vv0 + 4096;

  u32x4 qf[4];
  u32x4 kb[2][8], vb[2][8];
  auto load_k = [&](u32x4 (&k)[8], int it) {
    const unsigned char* base = Kp + it * (NWAVE * 8192);
    gld_nt<0>(k[0], vk0, base); gld_nt<64>(k[1], vk0, base); gld_nt<128>(k[2], vk0, base); gld_nt<192>(k[3], vk0, base);
    gld_nt<0>(k[4], vk1, base); gld_nt<64>(k[5], vk1, base); gld_nt<128>(k[6], vk1, base); gld_nt<192>(k[7], vk1, base);
  };
  auto load_v = [&](u32x4 (&v)[8], int it) {
    const unsigned char* base = Vp + it * (NWAVE * 8192);
    gld_nt<0>(v[0], vv0, base); gld_nt<256>(v[1], vv0, base); gld_nt<512>(v[2], vv0, base); gld_nt<768>(v[3], vv0, base);
    gld_nt<0>(v[4], vv1, base); gld_nt<256>(v[5], vv1, base); gld_nt<512>(v[6], vv1, base); gld_nt<768>(v[7], vv1, base);
  };
  auto load_wo = [&](u32x4 (&t)[8], int kk) {
    const unsigned char* base = reinterpret_cast<const unsigned char*>(wo + (size_t)(hkv * 512 + (w * 2 + kk) * 32) * DM + s * 128);
    const unsigned vo0 = g * 8192 + c16 * 16, vo1 = vo0 + 4096;
    gld<0>(t[0], vo0, base); gld<1024>(t[1], vo0, base); gld<2048>(t[2], vo0, base); gld<3072>(t[3], vo0, base);
    gld<0>(t[4], vo1, base); gld<1024>(t[5], vo1, base); gld<2048>(t[6], vo1, base); gld<3072>(t[7], vo1, base);
  };

  gld<0>(qf[0], vk0, Qp); gld<64>(qf[1], vk0, Qp); gld<128>(qf[2], vk0, Qp); gld<192>(qf[3], vk0, Qp);
  load_k(kb[0], 0);
  load_v(vb[0], 0);
  load_k(kb[1], 1);
  load_v(vb[1], 1);

  float m_run = -INFINITY, l_run = 0.f;
  f32x4 o[8];
#pragma unroll
  for (int i = 0; i < 8; ++i) o[i] = f32x4{0.f, 0.f, 0.f, 0.f};

#pragma unroll
  for (int it = 0; it < TILES_W; ++it) {
    u32x4 (&k)[8] = kb[it & 1];
    u32x4 (&v)[8] = vb[it & 1];
    wait_vm<24>();
    TIE8(k);
    if (it == 0) TIE4(qf);
    f32x4 s0 = f32x4{0.f, 0.f, 0.f, 0.f}, s1 = f32x4{0.f, 0.f, 0.f, 0.f};
#pragma unroll
    for (int c = 0; c < 4; ++c) {
      s0 = MFMA(__builtin_bit_cast(bf16x8, k[c]), __builtin_bit_cast(bf16x8, qf[c]), s0);
      s1 = MFMA(__builtin_bit_cast(bf16x8, k[4 + c]), __builtin_bit_cast(bf16x8, qf[c]), s1);
    }
    if (it + 2 < TILES_W) load_k(k, it + 2); else if (it + 2 == TILES_W) load_wo(k, 0);
    float tmax = fmaxf(fmaxf(fmaxf(s0[0], s0[1]), fmaxf(s0[2], s0[3])), fmaxf(fmaxf(s1[0], s1[1]), fmaxf(s1[2], s1[3])));
    tmax = xg_max(tmax);
    const float m_new = fmaxf(m_run, tmax);
    const float alpha = __builtin_amdgcn_exp2f((m_run - m_new) * SCALE_LOG2E);
    const float mb = m_new * SCALE_LOG2E;
    m_run = m_new;
    float p[8];
#pragma unroll
    for (int r = 0; r < 4; ++r) {
      p[r] = __builtin_amdgcn_exp2f(fmaf(s0[r], SCALE_LOG2E, -mb));
      p[4 + r] = __builtin_amdgcn_exp2f(fmaf(s1[r], SCALE_LOG2E, -mb));
    }
    l_run = fmaf(l_run, alpha, ((p[0] + p[1]) + (p[2] + p[3])) + ((p[4] + p[5]) + (p[6] + p[7])));
    u32x4 pf = {cvt_pk_bf16(p[0], p[1]), cvt_pk_bf16(p[2], p[3]), cvt_pk_bf16(p[4], p[5]), cvt_pk_bf16(p[6], p[7])};
    const bf16x8 pb = __builtin_bit_cast(bf16x8, pf);
#pragma unroll
    for (int i = 0; i < 8; ++i) o[i] = o[i] * alpha;
    __builtin_amdgcn_sched_barrier(0);
    if (it + 1 == TILES_W) wait_vm<16>(); else wait_vm<24>();
    TIE8(v);
#define PV(I) { o[I] = MFMA(frag_col<I>(v), pb, o[I]); }
    PV(0) PV(1) PV(2) PV(3) PV(4) PV(5) PV(6) PV(7)
#undef PV
    if (it + 2 < TILES_W) load_v(v, it + 2); else if (it + 2 == TILES_W) load_wo(v, 1);
  }
  STAMP(1);

  l_run = xg_sum(l_run);
  if (lane < 16) { sm_m[w * MROW + lane] = m_run; sm_l[w * MROW + lane] = l_run; }
  __syncthreads();
  STAMP(7);
  {
    float M = -INFINITY;
#pragma unroll
    for (int ww = 0; ww < NWAVE; ++ww) M = fmaxf(M, sm_m[ww * MROW + c16]);
    const float f = __builtin_amdgcn_exp2f((m_run - M) * SCALE_LOG2E);
#pragma unroll
    for (int r = 0; r < 4; ++r) {
      float* dst = sm_o + (w * MROW + c16) * 132 + g * 32 + r * 8;
      *reinterpret_cast<f32x4*>(dst) = f32x4{o[0][r] * f, o[1][r] * f, o[2][r] * f, o[3][r] * f};
      *reinterpret_cast<f32x4*>(dst + 4) = f32x4{o[4][r] * f, o[5][r] * f, o[6][r] * f, o[7][r] * f};
    }
  }
  __syncthreads();
  const int q = tid >> 5, d4 = tid & 31;
  f32x4 acc = *reinterpret_cast<const f32x4*>(sm_o + q * 132 + d4 * 4);
#pragma unroll
  for (int ww = 1; ww < NWAVE; ++ww) acc += *reinterpret_cast<const f32x4*>(sm_o + (ww * MROW + q) * 132 + d4 * 4);
  float Mq = -INFINITY;
#pragma unroll
  for (int ww = 0; ww < NWAVE; ++ww) Mq = fmaxf(Mq, sm_m[ww * MROW + q]);
  float Lq = 0.f;
#pragma unroll
  for (int ww = 0; ww < NWAVE; ++ww) Lq = fmaf(sm_l[ww * MROW + q], __builtin_amdgcn_exp2f((sm_m[ww * MROW + q] - Mq) * SCALE_LOG2E), Lq);
  Mq *= SCALE_LOG2E;
  st_wt16(ws + WS_PO + (((size_t)bid * MROW + q) * DH + d4 * 4) * 4, acc);
  if (d4 == 0) st_wt8(ws + WS_ML + ((size_t)bid * MROW + q) * 8, f32x2{Mq, Lq});
  wait_vm<0>();
  __syncthreads();
  unsigned* flags = reinterpret_cast<unsigned*>(ws + WS_FLAG) + bh * 32;
  if (tid == 0) __hip_atomic_store(flags + s, 1u, __ATOMIC_RELAXED, __HIP_MEMORY_SCOPE_AGENT);
  STAMP(2);
  if (w == 0) {
    for (unsigned spins = 0; spins < (1u << 24); ++spins) {
      unsigned f = 1u;
      if (lane < NSPLIT && lane != s) f = __hip_atomic_load(flags + lane, __ATOMIC_RELAXED, __HIP_MEMORY_SCOPE_AGENT);
      if (__ballot(f != 0u) == ~0ull) break;
      __builtin_amdgcn_s_sleep(1);
    }
  }
  __syncthreads();
  STAMP(3);
  {
    f32x4 ov[4];
    f32x2 ml[4];
#pragma unroll
    for (int sp = 0; sp < 4; ++sp) {
      const size_t wg = (size_t)(bid & ~(3 << 3)) | (sp << 3);
      ld_wt16(ov[sp], ws + WS_PO + ((wg * MROW + q) * DH + d4 * 4) * 4);
      ld_wt8(ml[sp], ws + WS_ML + (wg * MROW + q) * 8);
    }
    wait_vm<0>();
    asm volatile("" : "+v"(ov[0]), "+v"(ov[1]), "+v"(ov[2]), "+v"(ov[3]), "+v"(ml[0]), "+v"(ml[1]), "+v"(ml[2]), "+v"(ml[3]));
    const float M = fmaxf(fmaxf(ml[0][0], ml[1][0]), fmaxf(ml[2][0], ml[3][0]));
    float L = 0.f;
    f32x4 a = f32x4{0.f, 0.f, 0.f, 0.f};
#pragma unroll
    for (int sp = 0; sp < 4; ++sp) {
      const float wgt = __builtin_amdgcn_exp2f(ml[sp][0] - M);
      L = fmaf(wgt, ml[sp][1], L);
      a += ov[sp] * wgt;
    }
    const float inv = 1.f / L;
    const int j = q >> 2, sq = q & 3;
    u32x2 pk = {cvt_pk_bf16(a[0] * inv, a[1] * inv), cvt_pk_bf16(a[2] * inv, a[3] * inv)};
    *reinterpret_cast<u32x2*>(sA + sq * 520 + j * 128 + d4 * 4) = pk;
  }
  __syncthreads();
  wait_vm<0>();
  TIE8(kb[0]);
  TIE8(vb[0]);
  {
    f32x4 c8[8];
#pragma unroll
    for (int i = 0; i < 8; ++i) c8[i] = f32x4{0.f, 0.f, 0.f, 0.f};
    const bf16x8 a0 = *reinterpret_cast<const bf16x8*>(sA + (c16 & 3) * 520 + (w * 2) * 32 + g * 8);
    const bf16x8 a1 = *reinterpret_cast<const bf16x8*>(sA + (c16 & 3) * 520 + (w * 2 + 1) * 32 + g * 8);
#define OP(I) { c8[I] = MFMA(a0, frag_col<I>(kb[0]), c8[I]); c8[I] = MFMA(a1, frag_col<I>(vb[0]), c8[I]); }
    OP(0) OP(1) OP(2) OP(3) OP(4) OP(5) OP(6) OP(7)
#undef OP
    float* red = sm_o;
    if (g == 0) {
#pragma unroll
      for (int r = 0; r < 4; ++r) {
        float* dst = red + (w * 4 + r) * 132 + c16 * 8;
        *reinterpret_cast<f32x4*>(dst) = f32x4{c8[0][r], c8[1][r], c8[2][r], c8[3][r]};
        *reinterpret_cast<f32x4*>(dst + 4) = f32x4{c8[4][r], c8[5][r], c8[6][r], c8[7][r]};
      }
    }
  }
  __syncthreads();
  STAMP(4);
  const int grp = b * 4 + s;
  float* con = reinterpret_cast<float*>(ws + WS_CON) + (size_t)grp * HKV * 512;
  {
    const float* red = sm_o;
    float vsum = 0.f;
#pragma unroll
    for (int ww = 0; ww < NWAVE; ++ww) vsum += red[(ww * 4 + (tid >> 7)) * 132 + (tid & 127)];
    st_wt4(con + hkv * 512 + tid, vsum);
  }
  wait_vm<0>();
  __syncthreads();
  if (tid == 0) {
    unsigned* cnt = reinterpret_cast<unsigned*>(ws + WS_TICK) + grp * 16;
    const unsigned old = __hip_atomic_fetch_add(cnt, 1u, __ATOMIC_RELAXED, __HIP_MEMORY_SCOPE_AGENT);
    *s_last = (old == (unsigned)(HKV - 1)) ? 1u : 0u;
  }
  __syncthreads();
  STAMP(5);
  if (*s_last == 0u) return;
  {
    float cv[8];
#pragma unroll
    for (int hh = 0; hh < HKV; ++hh) ld_wt4(cv[hh], con + hh * 512 + tid);
    wait_vm<0>();
    asm volatile("" : "+v"(cv[0]), "+v"(cv[1]), "+v"(cv[2]), "+v"(cv[3]), "+v"(cv[4]), "+v"(cv[5]), "+v"(cv[6]), "+v"(cv[7]));
    float vsum = 0.f;
#pragma unroll
    for (int hh = 0; hh < HKV; ++hh) vsum += cv[hh];
    out[(size_t)(b * SQ + (tid >> 7)) * DM + s * 128 + (tid & 127)] = (u16)(cvt_pk_bf16(vsum, 0.f) & 0xffffu);
  }
  STAMP(6);
}

extern "C" void kernel_launch(void* const* d_in, const int* in_sizes, int n_in, void* d_out, int out_size, void* d_ws,
                              size_t ws_size, hipStream_t stream) {
  const u16* x = (const u16*)d_in[0];
  const u16* kc = (const u16*)d_in[1];
  const u16* vc = (const u16*)d_in[2];
  const u16* wq = (const u16*)d_in[3];
  const u16* wo = (const u16*)d_in[4];
  unsigned char* ws = (unsigned char*)d_ws;
  constexpr int ATT_LDS = (NWAVE * MROW * 132 + 2 * NWAVE * MROW) * 4 + 4 * 520 * 2 + 16;
  (void)hipFuncSetAttribute((const void*)attn_kernel, hipFuncAttributeMaxDynamicSharedMemorySize, ATT_LDS);
  qproj_kernel<<<256, 256, 0, stream>>>(x, wq, ws);
  attn_kernel<<<NB * HKV * NSPLIT, 512, ATT_LDS, stream>>>(kc, vc, wo, ws, (u16*)d_out);
}
```

```cpp
#include <hip/hip_runtime.h>
#include <hip/hip_bf16.h>
#include <stdint.h>

typedef __attribute__((ext_vector_type(8))) short bf16x8;
typedef __attribute__((ext_vector_type(4))) float f32x4;
typedef __attribute__((ext_vector_type(2))) float f32x2;
typedef __attribute__((ext_vector_type(2))) __bf16 bf16x2_t;
typedef __attribute__((ext_vector_type(4))) unsigned u32x4;
typedef __attribute__((ext_vector_type(2))) unsigned u32x2;
typedef unsigned short u16;

constexpr int NB = 8, SQ = 4, DM = 512, HQ = 32, HKV = 8, SK = 4096, DH = 128;
constexpr int MROW = 16;
constexpr int NSPLIT = 4;
constexpr int KEYS_WG = SK / NSPLIT;
constexpr int NWAVE = 8;
constexpr int TILES_W = KEYS_WG / 32 / NWAVE;
constexpr float SCALE_LOG2E = 0.08838834764831845f * 1.4426950408889634f;

constexpr size_t WS_FLAG = 0;
constexpr size_t WS_TICK = 8192;
constexpr int WS_ZERO_WORDS = (8192 + 2048) / 4;
constexpr size_t WS_Q = 16384;
constexpr size_t WS_ML = WS_Q + (size_t)NB * HKV * MROW * DH * 2;
constexpr size_t WS_PO = WS_ML + (size_t)NB * HKV * NSPLIT * MROW * 2 * 4;
constexpr size_t WS_CON = WS_PO + (size_t)NB * HKV * NSPLIT * MROW * DH * 4;

__device__ __forceinline__ unsigned cvt_pk_bf16(float lo, float hi) {
  f32x2 v = {lo, hi};
  return __builtin_bit_cast(unsigned, __builtin_convertvector(v, bf16x2_t));
}

__device__ __forceinline__ float xg_max(float x) {
  auto s = __builtin_amdgcn_permlane16_swap(__float_as_uint(x), __float_as_uint(x), false, false);
  x = fmaxf(__uint_as_float(s[0]), __uint_as_float(s[1]));
  auto t = __builtin_amdgcn_permlane32_swap(__float_as_uint(x), __float_as_uint(x), false, false);
  return fmaxf(__uint_as_float(t[0]), __uint_as_float(t[1]));
}
__device__ __forceinline__ float xg_sum(float x) {
  auto s = __builtin_amdgcn_permlane16_swap(__float_as_uint(x), __float_as_uint(x), false, false);
  x = __uint_as_float(s[0]) + __uint_as_float(s[1]);
  auto t = __builtin_amdgcn_permlane32_swap(__float_as_uint(x), __float_as_uint(x), false, false);
  return __uint_as_float(t[0]) + __uint_as_float(t[1]);
}

template <int I>
__device__ __forceinline__ bf16x8 frag_col(const u32x4 (&r)[8]) {
  constexpr unsigned sel = (I & 1) ? 0x07060302u : 0x05040100u;
  u32x4 f;
  f[0] = __builtin_amdgcn_perm(r[1][I >> 1], r[0][I >> 1], sel);
  f[1] = __builtin_amdgcn_perm(r[3][I >> 1], r[2][I >> 1], sel);
  f[2] = __builtin_amdgcn_perm(r[5][I >> 1], r[4][I >> 1], sel);
  f[3] = __builtin_amdgcn_perm(r[7][I >> 1], r[6][I >> 1], sel);
  return __builtin_bit_cast(bf16x8, f);
}

#define MFMA(a, b, c) __builtin_amdgcn_mfma_f32_16x16x32_bf16((a), (b), (c), 0, 0, 0)

template <int IMM>
__device__ __forceinline__ void gld_nt(u32x4& d, unsigned voff, const void* sbase) {
  asm volatile("global_load_dwordx4 %0, %1, %2 offset:%3 nt" : "=v"(d) : "v"(voff), "s"(sbase), "n"(IMM) : "memory");
}
template <int IMM>
__device__ __forceinline__ void gld(u32x4& d, unsigned voff, const void* sbase) {
  asm volatile("global_load_dwordx4 %0, %1, %2 offset:%3" : "=v"(d) : "v"(voff), "s"(sbase), "n"(IMM) : "memory");
}
template <int N> __device__ __forceinline__ void wait_vm() { asm volatile("s_waitcnt vmcnt(%0)" :: "n"(N) : "memory"); }
#define TIE8(a) asm volatile("" : "+v"(a[0]), "+v"(a[1]), "+v"(a[2]), "+v"(a[3]), "+v"(a[4]), "+v"(a[5]), "+v"(a[6]), "+v"(a[7]))
#define TIE4(a) asm volatile("" : "+v"(a[0]), "+v"(a[1]), "+v"(a[2]), "+v"(a[3]))

__device__ __forceinline__ void st_wt16(void* p, f32x4 v) {
  asm volatile("global_store_dwordx4 %0, %1, off sc1\n\ts_nop 1" :: "v"(p), "v"(v) : "memory");
}
__device__ __forceinline__ void st_wt8(void* p, f32x2 v) {
  asm volatile("global_store_dwordx2 %0, %1, off sc1\n\ts_nop 1" :: "v"(p), "v"(v) : "memory");
}
__device__ __forceinline__ void st_wt4(void* p, float v) {
  asm volatile("global_store_dword %0, %1, off sc1\n\ts_nop 1" :: "v"(p), "v"(v) : "memory");
}
__device__ __forceinline__ void ld_wt16(f32x4& d, const void* p) { asm volatile("global_load_dwordx4 %0, %1, off sc1" : "=v"(d) : "v"(p) : "memory"); }
__device__ __forceinline__ void ld_wt8(f32x2& d, const void* p) { asm volatile("global_load_dwordx2 %0, %1, off sc1" : "=v"(d) : "v"(p) : "memory"); }
__device__ __forceinline__ void ld_wt4(float& d, const void* p) { asm volatile("global_load_dword %0, %1, off sc1" : "=v"(d) : "v"(p) : "memory"); }

#define STAMP(i) do { } while (0)

__global__ __launch_bounds__(256) void qproj_kernel(const u16* __restrict__ x, const u16* __restrict__ wq, unsigned char* __restrict__ ws) {
  __shared__ __attribute__((aligned(16))) float red[4][2][64 * 4];
  const int tid = threadIdx.x, lane = tid & 63, w = tid >> 6, c16 = lane & 15, g = lane >> 4;
  const int bid = blockIdx.x;
  const int slab = (bid & 7) * 32 + (bid >> 3);
  const int n0 = slab * 16;
  {
    const int gid = bid * 256 + tid;
    if (gid < WS_ZERO_WORDS) reinterpret_cast<unsigned*>(ws)[gid] = 0u;
  }
  u16 wv[4][8];
#pragma unroll
  for (int ks = 0; ks < 4; ++ks)
#pragma unroll
    for (int j = 0; j < 8; ++j) wv[ks][j] = wq[(size_t)(w * 128 + ks * 32 + g * 8 + j) * (HQ * DH) + n0 + c16];
  bf16x8 xb[4][2];
#pragma unroll
  for (int ks = 0; ks < 4; ++ks)
#pragma unroll
    for (int mt = 0; mt < 2; ++mt) xb[ks][mt] = *reinterpret_cast<const bf16x8*>(x + (size_t)(mt * 16 + c16) * DM + w * 128 + ks * 32 + g * 8);
  f32x4 acc[2] = {f32x4{0.f, 0.f, 0.f, 0.f}, f32x4{0.f, 0.f, 0.f, 0.f}};
#pragma unroll
  for (int ks = 0; ks < 4; ++ks) {
    u32x4 a;
#pragma unroll
    for (int j = 0; j < 4; ++j) a[j] = (unsigned)wv[ks][2 * j] | ((unsigned)wv[ks][2 * j + 1] << 16);
    const bf16x8 ab = __builtin_bit_cast(bf16x8, a);
    acc[0] = MFMA(ab, xb[ks][0], acc[0]);
    acc[1] = MFMA(ab, xb[ks][1], acc[1]);
  }
  *reinterpret_cast<f32x4*>(&red[w][0][lane * 4]) = acc[0];
  *reinterpret_cast<f32x4*>(&red[w][1][lane * 4]) = acc[1];
  __syncthreads();
  {
    const int m = tid >> 3, np = tid & 7;
    const int mt = m >> 4, cm = m & 15, nl = np * 2, gg = nl >> 2, r = nl & 3;
    float v0 = 0.f, v1 = 0.f;
#pragma unroll
    for (int ww = 0; ww < 4; ++ww) {
      const f32x2 t = *reinterpret_cast<const f32x2*>(&red[ww][mt][(gg * 16 + cm) * 4 + r]);
      v0 += t[0]; v1 += t[1];
    }
    const int b = m >> 2, sq = m & 3;
    const int hkv = slab >> 5, j = (slab >> 3) & 3, d0 = (slab & 7) * 16;
    unsigned* q = reinterpret_cast<unsigned*>(ws + WS_Q) + ((((size_t)(b * HKV + hkv) * MROW + j * 4 + sq) * DH + d0 + nl) >> 1);
    *q = cvt_pk_bf16(v0, v1);
  }
}

__global__ __launch_bounds__(512, 2) void attn_kernel(const u16* __restrict__ Kc, const u16* __restrict__ Vc, const u16* __restrict__ wo,
                                                      unsigned char* __restrict__ ws, u16* __restrict__ out) {
  extern __shared__ __attribute__((aligned(16))) float smem[];
  float* sm_o = smem;
  float* sm_m = smem + NWAVE * MROW * 132;
  float* sm_l = sm_m + NWAVE * MROW;
  u16* sA = reinterpret_cast<u16*>(sm_l + NWAVE * MROW);
  unsigned* s_last = reinterpret_cast<unsigned*>(sA + 4 * 520);

  const int tid = threadIdx.x, lane = tid & 63, c16 = lane & 15, g = lane >> 4;
  const int w = __builtin_amdgcn_readfirstlane(tid >> 6);
  const int bid = blockIdx.x;
  const int hkv = bid & 7, b = bid >> 5, s = (bid >> 3) & 3;
  const int bh = b * HKV + hkv;
  STAMP(0);
  const unsigned char* Kp = reinterpret_cast<const unsigned char*>(Kc + ((size_t)bh * SK + s * KEYS_WG) * DH) + w * 8192;
  const unsigned char* Vp = reinterpret_cast<const unsigned char*>(Vc + ((size_t)bh * SK + s * KEYS_WG) * DH) + w * 8192;
  const unsigned char* Qp = ws + WS_Q + (size_t)bh * MROW * DH * 2;
  const unsigned vk0 = c16 * 256 + g * 16, vk1 = vk0 + 4096;
  const unsigned vv0 = g * 1024 + c16 * 16, vv1 = vv0 + 4096;

  u32x4 qf[4];
  u32x4 kb[2][8], vb[2][8];
  auto load_k = [&](u32x4 (&k)[8], int it) {
    const unsigned char* base = Kp + it * (NWAVE * 8192);
    gld_nt<0>(k[0], vk0, base); gld_nt<64>(k[1], vk0, base); gld_nt<128>(k[2], vk0, base); gld_nt<192>(k[3], vk0, base);
    gld_nt<0>(k[4], vk1, base); gld_nt<64>(k[5], vk1, base); gld_nt<128>(k[6], vk1, base); gld_nt<192>(k[7], vk1, base);
  };
  auto load_v = [&](u32x4 (&v)[8], int it) {
    const unsigned char* base = Vp + it * (NWAVE * 8192);
    gld_nt<0>(v[0], vv0, base); gld_nt<256>(v[1], vv0, base); gld_nt<512>(v[2], vv0, base); gld_nt<768>(v[3], vv0, base);
    gld_nt<0>(v[4], vv1, base); gld_nt<256>(v[5], vv1, base); gld_nt<512>(v[6], vv1, base); gld_nt<768>(v[7], vv1, base);
  };
  auto load_wo = [&](u32x4 (&t)[8], int kk) {
    const unsigned char* base = reinterpret_cast<const unsigned char*>(wo + (size_t)(hkv * 512 + (w * 2 + kk) * 32) * DM + s * 128);
    const unsigned vo0 = g * 8192 + c16 * 16, vo1 = vo0 + 4096;
    gld<0>(t[0], vo0, base); gld<1024>(t[1], vo0, base); gld<2048>(t[2], vo0, base); gld<3072>(t[3], vo0, base);
    gld<0>(t[4], vo1, base); gld<1024>(t[5], vo1, base); gld<2048>(t[6], vo1, base); gld<3072>(t[7], vo1, base);
  };

  gld<0>(qf[0], vk0, Qp); gld<64>(qf[1], vk0, Qp); gld<128>(qf[2], vk0, Qp); gld<192>(qf[3], vk0, Qp);
  load_k(kb[0], 0);
  load_v(vb[0], 0);
  load_k(kb[1], 1);
  load_v(vb[1], 1);

  float m_run = -INFINITY, l_run = 0.f;
  f32x4 o[8];
#pragma unroll
  for (int i = 0; i < 8; ++i) o[i] = f32x4{0.f, 0.f, 0.f, 0.f};

#pragma unroll
  for (int it = 0; it < TILES_W; ++it) {
    u32x4 (&k)[8] = kb[it & 1];
    u32x4 (&v)[8] = vb[it & 1];
    if (it == 3) wait_vm<8>(); else wait_vm<24>();
    TIE8(k);
    if (it == 0) TIE4(qf);
    f32x4 s0 = f32x4{0.f, 0.f, 0.f, 0.f}, s1 = f32x4{0.f, 0.f, 0.f, 0.f};
#pragma unroll
    for (int c = 0; c < 4; ++c) {
      s0 = MFMA(__builtin_bit_cast(bf16x8, k[c]), __builtin_bit_cast(bf16x8, qf[c]), s0);
      s1 = MFMA(__builtin_bit_cast(bf16x8, k[4 + c]), __builtin_bit_cast(bf16x8, qf[c]), s1);
    }
    if (it + 2 < TILES_W) load_k(k, it + 2);
    float tmax = fmaxf(fmaxf(fmaxf(s0[0], s0[1]), fmaxf(s0[2], s0[3])), fmaxf(fmaxf(s1[0], s1[1]), fmaxf(s1[2], s1[3])));
    tmax = xg_max(tmax);
    const float m_new = fmaxf(m_run, tmax);
    const float alpha = __builtin_amdgcn_exp2f((m_run - m_new) * SCALE_LOG2E);
    const float mb = m_new * SCALE_LOG2E;
    m_run = m_new;
    float p[8];
#pragma unroll
    for (int r = 0; r < 4; ++r) {
      p[r] = __builtin_amdgcn_exp2f(fmaf(s0[r], SCALE_LOG2E, -mb));
      p[4 + r] = __builtin_amdgcn_exp2f(fmaf(s1[r], SCALE_LOG2E, -mb));
    }
    l_run = fmaf(l_run, alpha, ((p[0] + p[1]) + (p[2] + p[3])) + ((p[4] + p[5]) + (p[6] + p[7])));
    u32x4 pf = {cvt_pk_bf16(p[0], p[1]), cvt_pk_bf16(p[2], p[3]), cvt_pk_bf16(p[4], p[5]), cvt_pk_bf16(p[6], p[7])};
    const bf16x8 pb = __builtin_bit_cast(bf16x8, pf);
#pragma unroll
    for (int i = 0; i < 8; ++i) o[i] = o[i] * alpha;
    __builtin_amdgcn_sched_barrier(0);
    if (it == 3) wait_vm<0>(); else if (it == 2) wait_vm<16>(); else wait_vm<24>();
    TIE8(v);
#define PV(I) { o[I] = MFMA(frag_col<I>(v), pb, o[I]); }
    PV(0) PV(1) PV(2) PV(3) PV(4) PV(5) PV(6) PV(7)
#undef PV
    if (it + 2 < TILES_W) load_v(v, it + 2);
  }
  STAMP(1);

  l_run = xg_sum(l_run);
  if (lane < 16) { sm_m[w * MROW + lane] = m_run; sm_l[w * MROW + lane] = l_run; }
  __syncthreads();
  STAMP(7);
  {
    float M = -INFINITY;
#pragma unroll
    for (int ww = 0; ww < NWAVE; ++ww) M = fmaxf(M, sm_m[ww * MROW + c16]);
    const float f = __builtin_amdgcn_exp2f((m_run - M) * SCALE_LOG2E);
#pragma unroll
    for (int r = 0; r < 4; ++r) {
      float* dst = sm_o + (w * MROW + c16) * 132 + g * 32 + r * 8;
      *reinterpret_cast<f32x4*>(dst) = f32x4{o[0][r] * f, o[1][r] * f, o[2][r] * f, o[3][r] * f};
      *reinterpret_cast<f32x4*>(dst + 4) = f32x4{o[4][r] * f, o[5][r] * f, o[6][r] * f, o[7][r] * f};
    }
  }
  __syncthreads();
  const int q = tid >> 5, d4 = tid & 31;
  f32x4 acc = *reinterpret_cast<const f32x4*>(sm_o + q * 132 + d4 * 4);
#pragma unroll
  for (int ww = 1; ww < NWAVE; ++ww) acc += *reinterpret_cast<const f32x4*>(sm_o + (ww * MROW + q) * 132 + d4 * 4);
  float Mq = -INFINITY;
#pragma unroll
  for (int ww = 0; ww < NWAVE; ++ww) Mq = fmaxf(Mq, sm_m[ww * MROW + q]);
  float Lq = 0.f;
#pragma unroll
  for (int ww = 0; ww < NWAVE; ++ww) Lq = fmaf(sm_l[ww * MROW + q], __builtin_amdgcn_exp2f((sm_m[ww * MROW + q] - Mq) * SCALE_LOG2E), Lq);
  Mq *= SCALE_LOG2E;
  st_wt16(ws + WS_PO + (((size_t)bid * MROW + q) * DH + d4 * 4) * 4, acc);
  if (d4 == 0) st_wt8(ws + WS_ML + ((size_t)bid * MROW + q) * 8, f32x2{Mq, Lq});
  if (w != 0) { load_wo(kb[0], 0); load_wo(vb[0], 1); wait_vm<16>(); } else { wait_vm<0>(); }
  __syncthreads();
  unsigned* flags = reinterpret_cast<unsigned*>(ws + WS_FLAG) + bh * 32;
  if (tid == 0) __hip_atomic_store(flags + s, 1u, __ATOMIC_RELAXED, __HIP_MEMORY_SCOPE_AGENT);
  STAMP(2);
  if (w == 0) {
    for (unsigned spins = 0; spins < (1u << 24); ++spins) {
      unsigned f = 1u;
      if (lane < NSPLIT && lane != s) f = __hip_atomic_load(flags + lane, __ATOMIC_RELAXED, __HIP_MEMORY_SCOPE_AGENT);
      if (__ballot(f != 0u) == ~0ull) break;
      __builtin_amdgcn_s_sleep(1);
    }
  }
  __syncthreads();
  STAMP(3);
  {
    f32x4 ov[4];
    f32x2 ml[4];
#pragma unroll
    for (int sp = 0; sp < 4; ++sp) {
      const size_t wg = (size_t)(bid & ~(3 << 3)) | (sp << 3);
      ld_wt16(ov[sp], ws + WS_PO + ((wg * MROW + q) * DH + d4 * 4) * 4);
      ld_wt8(ml[sp], ws + WS_ML + (wg * MROW + q) * 8);
    }
    if (w == 0) { load_wo(kb[0], 0); load_wo(vb[0], 1); wait_vm<16>(); } else { wait_vm<0>(); }
    asm volatile("" : "+v"(ov[0]), "+v"(ov[1]), "+v"(ov[2]), "+v"(ov[3]), "+v"(ml[0]), "+v"(ml[1]), "+v"(ml[2]), "+v"(ml[3]));
    const float M = fmaxf(fmaxf(ml[0][0], ml[1][0]), fmaxf(ml[2][0], ml[3][0]));
    float L = 0.f;
    f32x4 a = f32x4{0.f, 0.f, 0.f, 0.f};
#pragma unroll
    for (int sp = 0; sp < 4; ++sp) {
      const float wgt = __builtin_amdgcn_exp2f(ml[sp][0] - M);
      L = fmaf(wgt, ml[sp][1], L);
      a += ov[sp] * wgt;
    }
    const float inv = 1.f / L;
    const int j = q >> 2, sq = q & 3;
    u32x2 pk = {cvt_pk_bf16(a[0] * inv, a[1] * inv), cvt_pk_bf16(a[2] * inv, a[3] * inv)};
    *reinterpret_cast<u32x2*>(sA + sq * 520 + j * 128 + d4 * 4) = pk;
  }
  __syncthreads();
  wait_vm<0>();
  TIE8(kb[0]);
  TIE8(vb[0]);
  {
    f32x4 c8[8];
#pragma unroll
    for (int i = 0; i < 8; ++i) c8[i] = f32x4{0.f, 0.f, 0.f, 0.f};
    const bf16x8 a0 = *reinterpret_cast<const bf16x8*>(sA + (c16 & 3) * 520 + (w * 2) * 32 + g * 8);
    const bf16x8 a1 = *reinterpret_cast<const bf16x8*>(sA + (c16 & 3) * 520 + (w * 2 + 1) * 32 + g * 8);
#define OP(I) { c8[I] = MFMA(a0, frag_col<I>(kb[0]), c8[I]); c8[I] = MFMA(a1, frag_col<I>(vb[0]), c8[I]); }
    OP(0) OP(1) OP(2) OP(3) OP(4) OP(5) OP(6) OP(7)
#undef OP
    float* red = sm_o;
    if (g == 0) {
#pragma unroll
      for (int r = 0; r < 4; ++r) {
        float* dst = red + (w * 4 + r) * 132 + c16 * 8;
        *reinterpret_cast<f32x4*>(dst) = f32x4{c8[0][r], c8[1][r], c8[2][r], c8[3][r]};
        *reinterpret_cast<f32x4*>(dst + 4) = f32x4{c8[4][r], c8[5][r], c8[6][r], c8[7][r]};
      }
    }
  }
  __syncthreads();
  STAMP(4);
  const int grp = b * 4 + s;
  float* con = reinterpret_cast<float*>(ws + WS_CON) + (size_t)grp * HKV * 512;
  {
    const float* red = sm_o;
    float vsum = 0.f;
#pragma unroll
    for (int ww = 0; ww < NWAVE; ++ww) vsum += red[(ww * 4 + (tid >> 7)) * 132 + (tid & 127)];
    st_wt4(con + hkv * 512 + tid, vsum);
  }
  wait_vm<0>();
  __syncthreads();
  if (tid == 0) {
    unsigned* cnt = reinterpret_cast<unsigned*>(ws + WS_TICK) + grp * 16;
    const unsigned old = __hip_atomic_fetch_add(cnt, 1u, __ATOMIC_RELAXED, __HIP_MEMORY_SCOPE_AGENT);
    *s_last = (old == (unsigned)(HKV - 1)) ? 1u : 0u;
  }
  __syncthreads();
  STAMP(5);
  if (*s_last == 0u) return;
  {
    float cv[8];
#pragma unroll
    for (int hh = 0; hh < HKV; ++hh) ld_wt4(cv[hh], con + hh * 512 + tid);
    wait_vm<0>();
    asm volatile("" : "+v"(cv[0]), "+v"(cv[1]), "+v"(cv[2]), "+v"(cv[3]), "+v"(cv[4]), "+v"(cv[5]), "+v"(cv[6]), "+v"(cv[7]));
    float vsum = 0.f;
#pragma unroll
    for (int hh = 0; hh < HKV; ++hh) vsum += cv[hh];
    out[(size_t)(b * SQ + (tid >> 7)) * DM + s * 128 + (tid & 127)] = (u16)(cvt_pk_bf16(vsum, 0.f) & 0xffffu);
  }
  STAMP(6);
}

extern "C" void kernel_launch(void* const* d_in, const int* in_sizes, int n_in, void* d_out, int out_size, void* d_ws,
                              size_t ws_size, hipStream_t stream) {
  const u16* x = (const u16*)d_in[0];
  const u16* kc = (const u16*)d_in[1];
  const u16* vc = (const u16*)d_in[2];
  const u16* wq = (const u16*)d_in[3];
  const u16* wo = (const u16*)d_in[4];
  unsigned char* ws = (unsigned char*)d_ws;
  constexpr int ATT_LDS = (NWAVE * MROW * 132 + 2 * NWAVE * MROW) * 4 + 4 * 520 * 2 + 16;
  (void)hipFuncSetAttribute((const void*)attn_kernel, hipFuncAttributeMaxDynamicSharedMemorySize, ATT_LDS);
  qproj_kernel<<<256, 256, 0, stream>>>(x, wq, ws);
  attn_kernel<<<NB * HKV * NSPLIT, 512, ATT_LDS, stream>>>(kc, vc, wo, ws, (u16*)d_out);
}
```
